```python
import torch

def _softmax(x, axis=-1):
    return torch.softmax(x, dim=axis)
def _gelu(x):
    return torch.nn.functional.gelu(x, approximate="tanh")
def _silu(x):
    return torch.nn.functional.silu(x)
def _sigmoid(x):
    return torch.sigmoid(x)
def _layernorm(x, axis=-1, eps=1e-5):
    m = x.mean(dim=axis, keepdim=True)
    v = x.var(dim=axis, keepdim=True, unbiased=False)
    return (x - m) / torch.sqrt(v + eps)
def _scatter_add(tgt, idx, src, axis=0):
    return tgt.clone().index_add_(axis, idx, src)
def _segment_sum(src, seg, n):
    out = torch.zeros((n,) + src.shape[1:], dtype=src.dtype)
    return out.index_add_(0, seg, src)
def _pad(x, pw):
    flat = tuple(v for pair in reversed(pw) for v in pair)
    return torch.nn.functional.pad(x, flat)

kernel_name = 'standalone__standard_2d_1d_3d_conv__vision'

def setup_inputs():

    _w = (lambda a: (lambda t: t.to(torch.bfloat16) if t.is_floating_point() else t)(torch.as_tensor(a)))
    H = W = 384
    C = 192
    K = 1
    return {
        "x": _w((lambda *s: torch.randn(*s, dtype=torch.bfloat16) * 0.05)(1, H, W, C)),
        "w": _w((lambda *s: torch.randn(*s, dtype=torch.bfloat16) * 0.05)(K, K, C, C)),
        "b": _w((lambda *s: torch.randn(*s, dtype=torch.bfloat16) * 0.05)(C)),
    }

def reference(x, w, b):
    B, H, W, Cin = x.shape
    K = w.shape[0]
    pl = (K - 1) // 2
    pr = K // 2
    xp = _pad(x, ((0, 0), (pl, pr), (pl, pr), (0, 0)))
    out = sum(
        torch.einsum('bhwc,cd->bhwd', xp[:, i:i+H, j:j+W, :], w[i, j])
        for i in range(K) for j in range(K)
    )
    return out + b[None, None, None, :]
```

```cpp
#include <hip/hip_runtime.h>
#include <hip/hip_bf16.h>
#include <stdint.h>

typedef __attribute__((ext_vector_type(8))) __bf16 bf16x8;
typedef __attribute__((ext_vector_type(2))) __bf16 bf16x2;
typedef __attribute__((ext_vector_type(4))) float f32x4;
typedef __attribute__((ext_vector_type(2))) float f32x2;
typedef __attribute__((ext_vector_type(4))) unsigned u32x4;
typedef __attribute__((ext_vector_type(2))) unsigned u32x2;
#define LAS __attribute__((address_space(3)))
#define GAS __attribute__((address_space(1)))

#define LD_POL " nt"
#define ST_NT 0

constexpr int C = 192;
constexpr int ROWB = C * 2;
constexpr int TILEB = 16 * ROWB;
constexpr int NT = 9;
constexpr int NWG = 256;
constexpr int HALF = 4 * 3 * TILEB;
constexpr int M_FAST = NWG * 4 * NT * 16;

__device__ __forceinline__ unsigned pk_bf16(float a, float b) {
    f32x2 v = {a, b};
    bf16x2 r = __builtin_convertvector(v, bf16x2);
    return __builtin_bit_cast(unsigned, r);
}

__device__ __forceinline__ void dma_tile(const char* sbase, unsigned voff, unsigned voff2, unsigned ldsaddr) {
    asm volatile(
        "s_mov_b32 m0, %3\n\t"
        "s_nop 0\n\t"
        "global_load_lds_dwordx4 %0, %2" LD_POL "\n\t"
        "global_load_lds_dwordx4 %0, %2 offset:1024" LD_POL "\n\t"
        "global_load_lds_dwordx4 %0, %2 offset:2048" LD_POL "\n\t"
        "s_add_u32 m0, m0, 3072\n\t"
        "s_nop 0\n\t"
        "global_load_lds_dwordx4 %1, %2" LD_POL "\n\t"
        "global_load_lds_dwordx4 %1, %2 offset:1024" LD_POL "\n\t"
        "global_load_lds_dwordx4 %1, %2 offset:2048" LD_POL "\n\t"
        :
        : "v"(voff), "v"(voff2), "s"(sbase), "s"(ldsaddr)
        : "memory", "scc");
}
__device__ __forceinline__ void dma_w(const char* sbase, unsigned voff, unsigned voff2, unsigned ldsaddr) {
    asm volatile(
        "s_mov_b32 m0, %3\n\t"
        "s_nop 0\n\t"
        "global_load_lds_dwordx4 %0, %2\n\t"
        "global_load_lds_dwordx4 %0, %2 offset:1024\n\t"
        "global_load_lds_dwordx4 %0, %2 offset:2048\n\t"
        "s_add_u32 m0, m0, 3072\n\t"
        "s_nop 0\n\t"
        "global_load_lds_dwordx4 %1, %2\n\t"
        "global_load_lds_dwordx4 %1, %2 offset:1024\n\t"
        "global_load_lds_dwordx4 %1, %2 offset:2048\n\t"
        :
        : "v"(voff), "v"(voff2), "s"(sbase), "s"(ldsaddr)
        : "memory", "scc");
}
#define WAIT_VM(n) asm volatile("s_waitcnt vmcnt(" #n ")" ::: "memory")

__global__ void __launch_bounds__(256, 1)
conv1x1_fast(const char* __restrict__ x, const char* __restrict__ w, const char* __restrict__ bias, char* __restrict__ out) {
    __shared__ __attribute__((aligned(1024))) char smem[2 * HALF];
    LAS char* L = (LAS char*)smem;
    const unsigned lbase = (unsigned)(uintptr_t)L;
    const int tid = threadIdx.x;
    const int wave = __builtin_amdgcn_readfirstlane(tid >> 6);
    const int lane = tid & 63;
    const int c = lane & 15, q = lane >> 4;

    const size_t wrow0 = (size_t)(blockIdx.x * 4 + wave) * (NT * 16);
    const char* xw = x + wrow0 * ROWB;
    char* ow = out + wrow0 * ROWB;

    u32x4 b4; u32x2 b2;
    asm volatile("global_load_dwordx4 %0, %2, %4\n\tglobal_load_dwordx2 %1, %3, %4 offset:256"
                 : "=&v"(b4), "=&v"(b2) : "v"(c * 16), "v"(c * 8), "s"(bias) : "memory");
    const unsigned lin = lane * 16;
    const unsigned swz = ((lane & ~7) | ((lane ^ (lane >> 3)) & 7)) * 16;
#pragma unroll
    for (int i = 0; i < 3; ++i)
        dma_w(w + (wave * 3 + i) * TILEB, lin, lin + 3072, lbase + HALF + (wave * 3 + i) * TILEB);
#pragma unroll
    for (int i = 0; i < 3; ++i)
        dma_tile(xw + i * TILEB, swz, swz + 3072, lbase + (wave * 3 + i) * TILEB);
    asm volatile("s_waitcnt vmcnt(18)" : "+v"(b4), "+v"(b2) :: "memory");
    __builtin_amdgcn_s_barrier();
    asm volatile("" ::: "memory");

    u32x4 wf[6][12];
#pragma unroll
    for (int s = 0; s < 6; ++s) {
        u32x4 r4[8]; u32x2 r2[8];
#pragma unroll
        for (int j = 0; j < 8; ++j) {
            LAS char* p = L + HALF + (32 * s + 8 * q + j) * ROWB;
            r4[j] = *(LAS u32x4*)(p + 16 * c);
            r2[j] = *(LAS u32x2*)(p + 256 + 8 * c);
        }
#pragma unroll
        for (int t = 0; t < 8; ++t)
#pragma unroll
            for (int jj = 0; jj < 4; ++jj)
                wf[s][t][jj] = __builtin_amdgcn_perm(r4[2 * jj + 1][t >> 1], r4[2 * jj][t >> 1], (t & 1) ? 0x07060302u : 0x05040100u);
#pragma unroll
        for (int t = 0; t < 4; ++t)
#pragma unroll
            for (int jj = 0; jj < 4; ++jj)
                wf[s][8 + t][jj] = __builtin_amdgcn_perm(r2[2 * jj + 1][t >> 1], r2[2 * jj][t >> 1], (t & 1) ? 0x07060302u : 0x05040100u);
    }
    float bf[12];
#pragma unroll
    for (int t = 0; t < 8; ++t) bf[t] = __builtin_bit_cast(float, (t & 1) ? (b4[t >> 1] & 0xffff0000u) : (b4[t >> 1] << 16));
#pragma unroll
    for (int t = 0; t < 4; ++t) bf[8 + t] = __builtin_bit_cast(float, (t & 1) ? (b2[t >> 1] & 0xffff0000u) : (b2[t >> 1] << 16));

    asm volatile("s_waitcnt lgkmcnt(0)" ::: "memory");
    __builtin_amdgcn_s_barrier();
    asm volatile("" ::: "memory");
#pragma unroll
    for (int i = 0; i < 3; ++i)
        dma_tile(xw + (3 + i) * TILEB, swz, swz + 3072, lbase + HALF + (wave * 3 + i) * TILEB);

    unsigned aoff[6];
#pragma unroll
    for (int s = 0; s < 6; ++s) {
        const int line = 3 * c + (s >> 1);
        aoff[s] = line * 128 + (((4 * (s & 1) + q) ^ line) & 7) * 16;
    }
    const unsigned o4 = (4 * q) * ROWB + 16 * c;
    const unsigned o2 = (4 * q) * ROWB + 256 + 8 * c;

#pragma unroll 1
    for (int i = 0; i < NT; ++i) {
        switch (i) {
            case 0: WAIT_VM(30); break;
            case 1: WAIT_VM(38); break;
            case 2: WAIT_VM(46); break;
            case 3: WAIT_VM(54); break;
            case 4: WAIT_VM(56); break;
            case 5: WAIT_VM(58); break;
            case 6: WAIT_VM(60); break;
            case 7: WAIT_VM(54); break;
            default: WAIT_VM(48); break;
        }
        const int sl = (i >= 6) ? i - 6 : i;
        const unsigned slot = ((sl >= 3) ? HALF + (sl - 3) * TILEB : sl * TILEB) + wave * 3 * TILEB;
        bf16x8 a[6];
#pragma unroll
        for (int s = 0; s < 6; ++s) a[s] = *(LAS bf16x8*)(L + slot + aoff[s]);
        if (i < 3) {
            asm volatile("s_waitcnt lgkmcnt(0)" : "+v"(a[0]), "+v"(a[1]), "+v"(a[2]), "+v"(a[3]), "+v"(a[4]), "+v"(a[5]) :: "memory");
            dma_tile(xw + (6 + i) * TILEB, swz, swz + 3072, lbase + slot);
        }
        f32x4 acc[12];
#pragma unroll
        for (int t = 0; t < 12; ++t) acc[t] = f32x4{bf[t], bf[t], bf[t], bf[t]};
#pragma unroll
        for (int s = 0; s < 6; ++s)
#pragma unroll
            for (int t = 0; t < 12; ++t)
                acc[t] = __builtin_amdgcn_mfma_f32_16x16x32_bf16(a[s], __builtin_bit_cast(bf16x8, wf[s][t]), acc[t], 0, 0, 0);
        char* ot = ow + i * TILEB;
#pragma unroll
        for (int r = 0; r < 4; ++r) {
            u32x4 v4 = {pk_bf16(acc[0][r], acc[1][r]), pk_bf16(acc[2][r], acc[3][r]), pk_bf16(acc[4][r], acc[5][r]), pk_bf16(acc[6][r], acc[7][r])};
            u32x2 v2 = {pk_bf16(acc[8][r], acc[9][r]), pk_bf16(acc[10][r], acc[11][r])};
            *(GAS u32x4*)(ot + o4 + r * ROWB) = v4;
            *(GAS u32x2*)(ot + o2 + r * ROWB) = v2;
        }
    }
}

__global__ void conv1x1_generic(const __hip_bfloat16* __restrict__ x, const __hip_bfloat16* __restrict__ w,
                                const __hip_bfloat16* __restrict__ b, __hip_bfloat16* __restrict__ out, long total, int Cn) {
    for (long idx = (long)blockIdx.x * blockDim.x + threadIdx.x; idx < total; idx += (long)gridDim.x * blockDim.x) {
        const long m = idx / Cn; const int n = (int)(idx - m * Cn);
        float acc = 0.f;
        for (int k = 0; k < Cn; ++k) acc += __bfloat162float(x[m * Cn + k]) * __bfloat162float(w[(long)k * Cn + n]);
        out[idx] = __float2bfloat16(acc + __bfloat162float(b[n]));
    }
}

extern "C" void kernel_launch(void* const* d_in, const int* in_sizes, int n_in,
                              void* d_out, int out_size, void* d_ws, size_t ws_size,
                              hipStream_t stream) {
    const int Cn = in_sizes[2];
    if (Cn == C && in_sizes[0] == M_FAST * C && in_sizes[1] == C * C) {
        conv1x1_fast<<<NWG, 256, 0, stream>>>((const char*)d_in[0], (const char*)d_in[1], (const char*)d_in[2], (char*)d_out);
    } else {
        conv1x1_generic<<<2048, 256, 0, stream>>>((const __hip_bfloat16*)d_in[0], (const __hip_bfloat16*)d_in[1],
                                                   (const __hip_bfloat16*)d_in[2], (__hip_bfloat16*)d_out, (long)out_size, Cn);
    }
}
```

```cpp
#include <hip/hip_runtime.h>
#include <hip/hip_bf16.h>
#include <stdint.h>

typedef __attribute__((ext_vector_type(8))) __bf16 bf16x8;
typedef __attribute__((ext_vector_type(2))) __bf16 bf16x2;
typedef __attribute__((ext_vector_type(4))) float f32x4;
typedef __attribute__((ext_vector_type(2))) float f32x2;
typedef __attribute__((ext_vector_type(4))) unsigned u32x4;
typedef __attribute__((ext_vector_type(2))) unsigned u32x2;
#define LAS __attribute__((address_space(3)))
#define GAS __attribute__((address_space(1)))

#define LD_POL " nt"
#define ST_NT 1

constexpr int C = 192;
constexpr int ROWB = C * 2;
constexpr int TILEB = 16 * ROWB;
constexpr int NT = 9;
constexpr int NWG = 256;
constexpr int HALF = 4 * 3 * TILEB;
constexpr int M_FAST = NWG * 4 * NT * 16;

__device__ __forceinline__ unsigned pk_bf16(float a, float b) {
    f32x2 v = {a, b};
    bf16x2 r = __builtin_convertvector(v, bf16x2);
    return __builtin_bit_cast(unsigned, r);
}

__device__ __forceinline__ void dma_tile(const char* sbase, unsigned voff, unsigned voff2, unsigned ldsaddr) {
    asm volatile(
        "s_mov_b32 m0, %3\n\t"
        "s_nop 0\n\t"
        "global_load_lds_dwordx4 %0, %2" LD_POL "\n\t"
        "global_load_lds_dwordx4 %0, %2 offset:1024" LD_POL "\n\t"
        "global_load_lds_dwordx4 %0, %2 offset:2048" LD_POL "\n\t"
        "s_add_u32 m0, m0, 3072\n\t"
        "s_nop 0\n\t"
        "global_load_lds_dwordx4 %1, %2" LD_POL "\n\t"
        "global_load_lds_dwordx4 %1, %2 offset:1024" LD_POL "\n\t"
        "global_load_lds_dwordx4 %1, %2 offset:2048" LD_POL "\n\t"
        :
        : "v"(voff), "v"(voff2), "s"(sbase), "s"(ldsaddr)
        : "memory", "scc");
}
__device__ __forceinline__ void dma_w(const char* sbase, unsigned voff, unsigned voff2, unsigned ldsaddr) {
    asm volatile(
        "s_mov_b32 m0, %3\n\t"
        "s_nop 0\n\t"
        "global_load_lds_dwordx4 %0, %2\n\t"
        "global_load_lds_dwordx4 %0, %2 offset:1024\n\t"
        "global_load_lds_dwordx4 %0, %2 offset:2048\n\t"
        "s_add_u32 m0, m0, 3072\n\t"
        "s_nop 0\n\t"
        "global_load_lds_dwordx4 %1, %2\n\t"
        "global_load_lds_dwordx4 %1, %2 offset:1024\n\t"
        "global_load_lds_dwordx4 %1, %2 offset:2048\n\t"
        :
        : "v"(voff), "v"(voff2), "s"(sbase), "s"(ldsaddr)
        : "memory", "scc");
}
__device__ __forceinline__ unsigned long long rt() { unsigned long long t; asm volatile("s_memrealtime %0\n\ts_waitcnt lgkmcnt(0)" : "=s"(t) :: "memory"); return t; }
__device__ __forceinline__ unsigned long long ct() { unsigned long long t; asm volatile("s_memtime %0\n\ts_waitcnt lgkmcnt(0)" : "=s"(t) :: "memory"); return t; }
#define DIAG 3
#define WAIT_VM(n) asm volatile("s_waitcnt vmcnt(" #n ")" ::: "memory")

__global__ void __launch_bounds__(256, 1)
conv1x1_fast(const char* __restrict__ x, const char* __restrict__ w, const char* __restrict__ bias, char* __restrict__ out) {
    __shared__ __attribute__((aligned(1024))) char smem[2 * HALF];
    LAS char* L = (LAS char*)smem;
    const unsigned lbase = (unsigned)(uintptr_t)L;
    const unsigned long long t0 = rt(); const unsigned long long c0 = ct(); unsigned long long t1 = t0;
    const int tid = threadIdx.x;
    const int wave = __builtin_amdgcn_readfirstlane(tid >> 6);
    const int lane = tid & 63;
    const int c = lane & 15, q = lane >> 4;

    const size_t wrow0 = (size_t)(blockIdx.x * 4 + wave) * (NT * 16);
    const char* xw = x + wrow0 * ROWB;
    char* ow = out + wrow0 * ROWB;

    u32x4 b4; u32x2 b2;
    asm volatile("global_load_dwordx4 %0, %2, %4\n\tglobal_load_dwordx2 %1, %3, %4 offset:256"
                 : "=&v"(b4), "=&v"(b2) : "v"(c * 16), "v"(c * 8), "s"(bias) : "memory");
    const unsigned lin = lane * 16;
    const unsigned swz = ((lane & ~7) | ((lane ^ (lane >> 3)) & 7)) * 16;
#pragma unroll
    for (int i = 0; i < 3; ++i)
        dma_w(w + (wave * 3 + i) * TILEB, lin, lin + 3072, lbase + HALF + (wave * 3 + i) * TILEB);
#pragma unroll
    for (int i = 0; i < 3; ++i)
        dma_tile(xw + i * TILEB, swz, swz + 3072, lbase + (wave * 3 + i) * TILEB);
    asm volatile("s_waitcnt vmcnt(18)" : "+v"(b4), "+v"(b2) :: "memory");
    if (DIAG == 3) t1 = rt();
    __builtin_amdgcn_s_barrier();
    asm volatile("" ::: "memory");

    u32x4 wf[6][12];
#pragma unroll
    for (int s = 0; s < 6; ++s) {
        u32x4 r4[8]; u32x2 r2[8];
#pragma unroll
        for (int j = 0; j < 8; ++j) {
            LAS char* p = L + HALF + (32 * s + 8 * q + j) * ROWB;
            r4[j] = *(LAS u32x4*)(p + 16 * c);
            r2[j] = *(LAS u32x2*)(p + 256 + 8 * c);
        }
#pragma unroll
        for (int t = 0; t < 8; ++t)
#pragma unroll
            for (int jj = 0; jj < 4; ++jj)
                wf[s][t][jj] = __builtin_amdgcn_perm(r4[2 * jj + 1][t >> 1], r4[2 * jj][t >> 1], (t & 1) ? 0x07060302u : 0x05040100u);
#pragma unroll
        for (int t = 0; t < 4; ++t)
#pragma unroll
            for (int jj = 0; jj < 4; ++jj)
                wf[s][8 + t][jj] = __builtin_amdgcn_perm(r2[2 * jj + 1][t >> 1], r2[2 * jj][t >> 1], (t & 1) ? 0x07060302u : 0x05040100u);
    }
    float bf[12];
#pragma unroll
    for (int t = 0; t < 8; ++t) bf[t] = __builtin_bit_cast(float, (t & 1) ? (b4[t >> 1] & 0xffff0000u) : (b4[t >> 1] << 16));
#pragma unroll
    for (int t = 0; t < 4; ++t) bf[8 + t] = __builtin_bit_cast(float, (t & 1) ? (b2[t >> 1] & 0xffff0000u) : (b2[t >> 1] << 16));

    asm volatile("s_waitcnt lgkmcnt(0)" ::: "memory");
    __builtin_amdgcn_s_barrier();
    if (DIAG == 2) t1 = rt();
    asm volatile("" ::: "memory");
#pragma unroll
    for (int i = 0; i < 3; ++i)
        dma_tile(xw + (3 + i) * TILEB, swz, swz + 3072, lbase + HALF + (wave * 3 + i) * TILEB);

    unsigned aoff[6];
#pragma unroll
    for (int s = 0; s < 6; ++s) {
        const int line = 3 * c + (s >> 1);
        aoff[s] = line * 128 + (((4 * (s & 1) + q) ^ line) & 7) * 16;
    }
    const unsigned o4 = (4 * q) * ROWB + 16 * c;
    const unsigned o2 = (4 * q) * ROWB + 256 + 8 * c;

#pragma unroll 1
    for (int i = 0; i < NT; ++i) {
        switch (i) {
            case 0: WAIT_VM(30); break;
            case 1: WAIT_VM(38); break;
            case 2: WAIT_VM(46); break;
            case 3: WAIT_VM(54); break;
            case 4: WAIT_VM(56); break;
            case 5: WAIT_VM(58); break;
            case 6: WAIT_VM(60); break;
            case 7: WAIT_VM(54); break;
            default: WAIT_VM(48); break;
        }
        if (DIAG == 5 && i == 8) t1 = rt();
        if (DIAG == 6 && i == 0) t1 = rt();
        const int sl = (i >= 6) ? i - 6 : i;
        const unsigned slot = ((sl >= 3) ? HALF + (sl - 3) * TILEB : sl * TILEB) + wave * 3 * TILEB;
        bf16x8 a[6];
#pragma unroll
        for (int s = 0; s < 6; ++s) a[s] = *(LAS bf16x8*)(L + slot + aoff[s]);
        if (i < 3) {
            asm volatile("s_waitcnt lgkmcnt(0)" : "+v"(a[0]), "+v"(a[1]), "+v"(a[2]), "+v"(a[3]), "+v"(a[4]), "+v"(a[5]) :: "memory");
            dma_tile(xw + (6 + i) * TILEB, swz, swz + 3072, lbase + slot);
        }
        f32x4 acc[12];
#pragma unroll
        for (int t = 0; t < 12; ++t) acc[t] = f32x4{bf[t], bf[t], bf[t], bf[t]};
#pragma unroll
        for (int s = 0; s < 6; ++s)
#pragma unroll
            for (int t = 0; t < 12; ++t)
                acc[t] = __builtin_amdgcn_mfma_f32_16x16x32_bf16(a[s], __builtin_bit_cast(bf16x8, wf[s][t]), acc[t], 0, 0, 0);
        char* ot = ow + i * TILEB;
#pragma unroll
        for (int r = 0; r < 4; ++r) {
            u32x4 v4 = {pk_bf16(acc[0][r], acc[1][r]), pk_bf16(acc[2][r], acc[3][r]), pk_bf16(acc[4][r], acc[5][r]), pk_bf16(acc[6][r], acc[7][r])};
            u32x2 v2 = {pk_bf16(acc[8][r], acc[9][r]), pk_bf16(acc[10][r], acc[11][r])};
            __builtin_nontemporal_store(v4, (GAS u32x4*)(ot + o4 + r * ROWB));
            __builtin_nontemporal_store(v2, (GAS u32x2*)(ot + o2 + r * ROWB));
        }
    }
    WAIT_VM(0);
    {
        const unsigned long long te = rt(); const unsigned long long ce = ct();
        unsigned long long extra = 0;
        if (DIAG == 1) extra = te - t0;
        if (DIAG == 2 || DIAG == 3 || DIAG == 6) extra = 4 * (t1 - t0);
        if (DIAG == 4) extra = (unsigned)(ce - c0) / 24u;
        if (DIAG == 5) extra = (t1 - t0);
        if (extra > 20000) extra = 20000;
        while (rt() - te < extra) __builtin_amdgcn_s_sleep(2);
    }
}

__global__ void conv1x1_generic(const __hip_bfloat16* __restrict__ x, const __hip_bfloat16* __restrict__ w,
                                const __hip_bfloat16* __restrict__ b, __hip_bfloat16* __restrict__ out, long total, int Cn) {
    for (long idx = (long)blockIdx.x * blockDim.x + threadIdx.x; idx < total; idx += (long)gridDim.x * blockDim.x) {
        const long m = idx / Cn; const int n = (int)(idx - m * Cn);
        float acc = 0.f;
        for (int k = 0; k < Cn; ++k) acc += __bfloat162float(x[m * Cn + k]) * __bfloat162float(w[(long)k * Cn + n]);
        out[idx] = __float2bfloat16(acc + __bfloat162float(b[n]));
    }
}

extern "C" void kernel_launch(void* const* d_in, const int* in_sizes, int n_in,
                              void* d_out, int out_size, void* d_ws, size_t ws_size,
                              hipStream_t stream) {
    const int Cn = in_sizes[2];
    if (Cn == C && in_sizes[0] == M_FAST * C && in_sizes[1] == C * C) {
        conv1x1_fast<<<NWG, 256, 0, stream>>>((const char*)d_in[0], (const char*)d_in[1], (const char*)d_in[2], (char*)d_out);
    } else {
        conv1x1_generic<<<2048, 256, 0, stream>>>((const __hip_bfloat16*)d_in[0], (const __hip_bfloat16*)d_in[1],
                                                   (const __hip_bfloat16*)d_in[2], (__hip_bfloat16*)d_out, (long)out_size, Cn);
    }
}
```

```cpp
#include <hip/hip_runtime.h>
#include <hip/hip_bf16.h>
#include <stdint.h>

typedef __attribute__((ext_vector_type(8))) __bf16 bf16x8;
typedef __attribute__((ext_vector_type(2))) __bf16 bf16x2;
typedef __attribute__((ext_vector_type(4))) float f32x4;
typedef __attribute__((ext_vector_type(2))) float f32x2;
typedef __attribute__((ext_vector_type(4))) unsigned u32x4;
typedef __attribute__((ext_vector_type(2))) unsigned u32x2;
#define LAS __attribute__((address_space(3)))
#define GAS __attribute__((address_space(1)))

#define LD_POL " nt"
#define ST_NT 1

constexpr int C = 192;
constexpr int ROWB = C * 2;
constexpr int TILEB = 16 * ROWB;
constexpr int NT = 9;
constexpr int NWG = 256;
constexpr int HALF = 4 * 3 * TILEB;
constexpr int M_FAST = NWG * 4 * NT * 16;

__device__ __forceinline__ unsigned pk_bf16(float a, float b) {
    f32x2 v = {a, b};
    bf16x2 r = __builtin_convertvector(v, bf16x2);
    return __builtin_bit_cast(unsigned, r);
}

__device__ __forceinline__ void dma_tile(const char* sbase, unsigned voff, unsigned voff2, unsigned ldsaddr) {
    asm volatile(
        "s_mov_b32 m0, %3\n\t"
        "s_nop 0\n\t"
        "global_load_lds_dwordx4 %0, %2" LD_POL "\n\t"
        "global_load_lds_dwordx4 %0, %2 offset:1024" LD_POL "\n\t"
        "global_load_lds_dwordx4 %0, %2 offset:2048" LD_POL "\n\t"
        "s_add_u32 m0, m0, 3072\n\t"
        "s_nop 0\n\t"
        "global_load_lds_dwordx4 %1, %2" LD_POL "\n\t"
        "global_load_lds_dwordx4 %1, %2 offset:1024" LD_POL "\n\t"
        "global_load_lds_dwordx4 %1, %2 offset:2048" LD_POL "\n\t"
        :
        : "v"(voff), "v"(voff2), "s"(sbase), "s"(ldsaddr)
        : "memory", "scc");
}
__device__ __forceinline__ void dma_w(const char* sbase, unsigned voff, unsigned voff2, unsigned ldsaddr) {
    asm volatile(
        "s_mov_b32 m0, %3\n\t"
        "s_nop 0\n\t"
        "global_load_lds_dwordx4 %0, %2\n\t"
        "global_load_lds_dwordx4 %0, %2 offset:1024\n\t"
        "global_load_lds_dwordx4 %0, %2 offset:2048\n\t"
        "s_add_u32 m0, m0, 3072\n\t"
        "s_nop 0\n\t"
        "global_load_lds_dwordx4 %1, %2\n\t"
        "global_load_lds_dwordx4 %1, %2 offset:1024\n\t"
        "global_load_lds_dwordx4 %1, %2 offset:2048\n\t"
        :
        : "v"(voff), "v"(voff2), "s"(sbase), "s"(ldsaddr)
        : "memory", "scc");
}
__device__ __forceinline__ unsigned long long rt() { unsigned long long t; asm volatile("s_memrealtime %0\n\ts_waitcnt lgkmcnt(0)" : "=s"(t) :: "memory"); return t; }
__device__ __forceinline__ unsigned long long ct() { unsigned long long t; asm volatile("s_memtime %0\n\ts_waitcnt lgkmcnt(0)" : "=s"(t) :: "memory"); return t; }
#define DIAG 0
#define ST_AUX 17
#define WAIT_VM(n) asm volatile("s_waitcnt vmcnt(" #n ")" ::: "memory")

__global__ void __launch_bounds__(256, 1)
conv1x1_fast(const char* __restrict__ x, const char* __restrict__ w, const char* __restrict__ bias, char* __restrict__ out) {
    __shared__ __attribute__((aligned(1024))) char smem[2 * HALF];
    LAS char* L = (LAS char*)smem;
    const unsigned lbase = (unsigned)(uintptr_t)L;
    const unsigned long long t0 = rt(); const unsigned long long c0 = ct(); unsigned long long t1 = t0;
    const int tid = threadIdx.x;
    const int wave = __builtin_amdgcn_readfirstlane(tid >> 6);
    const int lane = tid & 63;
    const int c = lane & 15, q = lane >> 4;

    const size_t wrow0 = (size_t)(blockIdx.x * 4 + wave) * (NT * 16);
    const char* xw = x + wrow0 * ROWB;
    char* ow = out + wrow0 * ROWB;

    u32x4 b4; u32x2 b2;
    asm volatile("global_load_dwordx4 %0, %2, %4\n\tglobal_load_dwordx2 %1, %3, %4 offset:256"
                 : "=&v"(b4), "=&v"(b2) : "v"(c * 16), "v"(c * 8), "s"(bias) : "memory");
    const unsigned lin = lane * 16;
    const unsigned swz = ((lane & ~7) | ((lane ^ (lane >> 3)) & 7)) * 16;
#pragma unroll
    for (int i = 0; i < 3; ++i)
        dma_w(w + (wave * 3 + i) * TILEB, lin, lin + 3072, lbase + HALF + (wave * 3 + i) * TILEB);
#pragma unroll
    for (int i = 0; i < 3; ++i)
        dma_tile(xw + i * TILEB, swz, swz + 3072, lbase + (wave * 3 + i) * TILEB);
    asm volatile("s_waitcnt vmcnt(18)" : "+v"(b4), "+v"(b2) :: "memory");
    if (DIAG == 3) t1 = rt();
    __builtin_amdgcn_s_barrier();
    asm volatile("" ::: "memory");

    u32x4 wf[6][12];
#pragma unroll
    for (int s = 0; s < 6; ++s) {
        u32x4 r4[8]; u32x2 r2[8];
#pragma unroll
        for (int j = 0; j < 8; ++j) {
            LAS char* p = L + HALF + (32 * s + 8 * q + j) * ROWB;
            r4[j] = *(LAS u32x4*)(p + 16 * c);
            r2[j] = *(LAS u32x2*)(p + 256 + 8 * c);
        }
#pragma unroll
        for (int t = 0; t < 8; ++t)
#pragma unroll
            for (int jj = 0; jj < 4; ++jj)
                wf[s][t][jj] = __builtin_amdgcn_perm(r4[2 * jj + 1][t >> 1], r4[2 * jj][t >> 1], (t & 1) ? 0x07060302u : 0x05040100u);
#pragma unroll
        for (int t = 0; t < 4; ++t)
#pragma unroll
            for (int jj = 0; jj < 4; ++jj)
                wf[s][8 + t][jj] = __builtin_amdgcn_perm(r2[2 * jj + 1][t >> 1], r2[2 * jj][t >> 1], (t & 1) ? 0x07060302u : 0x05040100u);
    }
    float bf[12];
#pragma unroll
    for (int t = 0; t < 8; ++t) bf[t] = __builtin_bit_cast(float, (t & 1) ? (b4[t >> 1] & 0xffff0000u) : (b4[t >> 1] << 16));
#pragma unroll
    for (int t = 0; t < 4; ++t) bf[8 + t] = __builtin_bit_cast(float, (t & 1) ? (b2[t >> 1] & 0xffff0000u) : (b2[t >> 1] << 16));

    asm volatile("s_waitcnt lgkmcnt(0)" ::: "memory");
    __builtin_amdgcn_s_barrier();
    if (DIAG == 2) t1 = rt();
    asm volatile("" ::: "memory");
#pragma unroll
    for (int i = 0; i < 3; ++i)
        dma_tile(xw + (3 + i) * TILEB, swz, swz + 3072, lbase + HALF + (wave * 3 + i) * TILEB);

    unsigned aoff[6];
#pragma unroll
    for (int s = 0; s < 6; ++s) {
        const int line = 3 * c + (s >> 1);
        aoff[s] = line * 128 + (((4 * (s & 1) + q) ^ line) & 7) * 16;
    }
    const __amdgpu_buffer_rsrc_t orsrc = __builtin_amdgcn_make_buffer_rsrc(ow, 0, NT * TILEB, 0x00020000);
    const unsigned o4 = (4 * q) * ROWB + 16 * c;
    const unsigned o2 = (4 * q) * ROWB + 256 + 8 * c;

#pragma unroll 1
    for (int i = 0; i < NT; ++i) {
        switch (i) {
            case 0: WAIT_VM(30); break;
            case 1: WAIT_VM(38); break;
            case 2: WAIT_VM(46); break;
            case 3: WAIT_VM(54); break;
            case 4: WAIT_VM(56); break;
            case 5: WAIT_VM(58); break;
            case 6: WAIT_VM(60); break;
            case 7: WAIT_VM(54); break;
            default: WAIT_VM(48); break;
        }
        if (DIAG == 5 && i == 8) t1 = rt();
        if (DIAG == 6 && i == 0) t1 = rt();
        const int sl = (i >= 6) ? i - 6 : i;
        const unsigned slot = ((sl >= 3) ? HALF + (sl - 3) * TILEB : sl * TILEB) + wave * 3 * TILEB;
        bf16x8 a[6];
#pragma unroll
        for (int s = 0; s < 6; ++s) a[s] = *(LAS bf16x8*)(L + slot + aoff[s]);
        if (i < 3) {
            asm volatile("s_waitcnt lgkmcnt(0)" : "+v"(a[0]), "+v"(a[1]), "+v"(a[2]), "+v"(a[3]), "+v"(a[4]), "+v"(a[5]) :: "memory");
            dma_tile(xw + (6 + i) * TILEB, swz, swz + 3072, lbase + slot);
        }
        f32x4 acc[12];
#pragma unroll
        for (int t = 0; t < 12; ++t) acc[t] = f32x4{bf[t], bf[t], bf[t], bf[t]};
#pragma unroll
        for (int s = 0; s < 6; ++s)
#pragma unroll
            for (int t = 0; t < 12; ++t)
                acc[t] = __builtin_amdgcn_mfma_f32_16x16x32_bf16(a[s], __builtin_bit_cast(bf16x8, wf[s][t]), acc[t], 0, 0, 0);
#pragma unroll
        for (int r = 0; r < 4; ++r) {
            u32x4 v4 = {pk_bf16(acc[0][r], acc[1][r]), pk_bf16(acc[2][r], acc[3][r]), pk_bf16(acc[4][r], acc[5][r]), pk_bf16(acc[6][r], acc[7][r])};
            u32x2 v2 = {pk_bf16(acc[8][r], acc[9][r]), pk_bf16(acc[10][r], acc[11][r])};
            __builtin_amdgcn_raw_buffer_store_b128(v4, orsrc, o4 + r * ROWB, i * TILEB, ST_AUX);
            __builtin_amdgcn_raw_buffer_store_b64(v2, orsrc, o2 + r * ROWB, i * TILEB, ST_AUX);
        }
    }
    if (DIAG) {
        WAIT_VM(0);
        const unsigned long long te = rt(); const unsigned long long ce = ct();
        unsigned long long extra = 0;
        if (DIAG == 1) extra = te - t0;
        if (DIAG == 2 || DIAG == 3 || DIAG == 6) extra = 4 * (t1 - t0);
        if (DIAG == 4) extra = (unsigned)(ce - c0) / 24u;
        if (DIAG == 5) extra = (t1 - t0);
        if (DIAG == 8) { asm volatile("buffer_wbl2 sc1\n\ts_waitcnt vmcnt(0)" ::: "memory"); }
        if (DIAG == 7 || DIAG == 8) extra = 4000 - (te - t0);
        if (extra > 20000) extra = 20000;
        while (rt() - te < extra) __builtin_amdgcn_s_sleep(2);
    }
}

__global__ void conv1x1_generic(const __hip_bfloat16* __restrict__ x, const __hip_bfloat16* __restrict__ w,
                                const __hip_bfloat16* __restrict__ b, __hip_bfloat16* __restrict__ out, long total, int Cn) {
    for (long idx = (long)blockIdx.x * blockDim.x + threadIdx.x; idx < total; idx += (long)gridDim.x * blockDim.x) {
        const long m = idx / Cn; const int n = (int)(idx - m * Cn);
        float acc = 0.f;
        for (int k = 0; k < Cn; ++k) acc += __bfloat162float(x[m * Cn + k]) * __bfloat162float(w[(long)k * Cn + n]);
        out[idx] = __float2bfloat16(acc + __bfloat162float(b[n]));
    }
}

extern "C" void kernel_launch(void* const* d_in, const int* in_sizes, int n_in,
                              void* d_out, int out_size, void* d_ws, size_t ws_size,
                              hipStream_t stream) {
    const int Cn = in_sizes[2];
    if (Cn == C && in_sizes[0] == M_FAST * C && in_sizes[1] == C * C) {
        conv1x1_fast<<<NWG, 256, 0, stream>>>((const char*)d_in[0], (const char*)d_in[1], (const char*)d_in[2], (char*)d_out);
    } else {
        conv1x1_generic<<<2048, 256, 0, stream>>>((const __hip_bfloat16*)d_in[0], (const __hip_bfloat16*)d_in[1],
                                                   (const __hip_bfloat16*)d_in[2], (__hip_bfloat16*)d_out, (long)out_size, Cn);
    }
}
```

```cpp
#include <hip/hip_runtime.h>
#include <hip/hip_bf16.h>
#include <stdint.h>

typedef __attribute__((ext_vector_type(8))) __bf16 bf16x8;
typedef __attribute__((ext_vector_type(2))) __bf16 bf16x2;
typedef __attribute__((ext_vector_type(4))) float f32x4;
typedef __attribute__((ext_vector_type(2))) float f32x2;
typedef __attribute__((ext_vector_type(4))) unsigned u32x4;
typedef __attribute__((ext_vector_type(2))) unsigned u32x2;
#define LAS __attribute__((address_space(3)))
#define GAS __attribute__((address_space(1)))

constexpr int C = 192;
constexpr int ROWB = C * 2;
constexpr int TILEB = 16 * ROWB;
constexpr int NT = 36;
constexpr int NWG = 256;
constexpr int G = 3;
constexpr int NCONS = 3 * G;
constexpr int NTHREADS = (NCONS + 2) * 64;
constexpr int RING = 26;
constexpr int WSLOT = RING - 12;
constexpr int WOFF = WSLOT * TILEB;
constexpr int LEAD = WSLOT;
constexpr int FLAGS = RING * TILEB;
constexpr int M_FAST = NWG * NT * 16;
constexpr int DEPTH = 6;
constexpr int NFREE = 7;
constexpr int TAU_LD = 35;
constexpr int EXTRA = 3;
constexpr int TAU_C = 31;
constexpr int SLOW = 40;
constexpr int NPACED = 26;
constexpr int TAU_MIN = 24;
constexpr int ALLOW = 200;
constexpr int ROUNDS = 4;
constexpr int CONF = 120;
constexpr int GSZ = 38;
constexpr int K1 = 12;
constexpr int K3 = 28;
static_assert(K1 % 2 == 0 && 2 * NT - K1 + EXTRA <= 64 && 2 * K1 % 4 == 0 && DEPTH <= 6 && K3 < 32 && NT > 32 && LEAD > G && LEAD <= WSLOT, "");

__device__ __forceinline__ unsigned pk_bf16(float a, float b) {
    f32x2 v = {a, b};
    return __builtin_bit_cast(unsigned, __builtin_convertvector(v, bf16x2));
}
__device__ __forceinline__ void dma_1k(const char* sbase, unsigned voff, unsigned ldsaddr) {
    asm volatile("s_mov_b32 m0, %2\n\ts_nop 0\n\tglobal_load_lds_dwordx4 %0, %1" :: "v"(voff), "s"(sbase), "s"(ldsaddr) : "memory");
}
__device__ __forceinline__ void lds_st(unsigned addr, unsigned val) {
    asm volatile("ds_write_b32 %0, %1" :: "v"(addr), "v"(val) : "memory");
}
__device__ __forceinline__ unsigned lds_ld(unsigned addr) {
    unsigned v;
    asm volatile("ds_read_b32 %0, %1\n\ts_waitcnt lgkmcnt(0)" : "=v"(v) : "v"(addr) : "memory");
    return v;
}
template <int OFF> __device__ __forceinline__ void lds_ld64(u32x2& v, unsigned addr) {
    asm volatile("ds_read_b64 %0, %1 offset:%2" : "=v"(v) : "v"(addr), "n"(OFF) : "memory");
}

__global__ void __launch_bounds__(NTHREADS, 3) __attribute__((aligned(4096)))
conv1x1_fast(const char* __restrict__ x, const char* __restrict__ w, const char* __restrict__ bias, char* __restrict__ out, char* __restrict__ wsp) {
    __shared__ __attribute__((aligned(1024))) char smem[FLAGS + 512];
    const unsigned lbase = (unsigned)(uintptr_t)(LAS char*)smem;
    const int wave = __builtin_amdgcn_readfirstlane(threadIdx.x >> 6);
    const unsigned lane = threadIdx.x & 63;
    const unsigned full_addr = lbase + FLAGS;
    const unsigned pair = (blockIdx.x >> 3) * 4 + (blockIdx.x & 3), role = (blockIdx.x >> 2) & 1;
    const unsigned grp = pair / GSZ, gsz = min((unsigned)GSZ, 128u - grp * GSZ);
    const unsigned blk0 = grp * (8 * GSZ) + pair;
    unsigned lo, swz, cdv, cv0;
    {
        const unsigned i = lane < NT ? lane : (lane & 15) + 8, odd = i & 1;
        unsigned t0 = (i >> 1) * 8 + 1 + odd, t1 = ((i - K1) >> 1) * 8 + 3 * odd, t2 = (i >> 2) * 8 + (i & 3);
        asm volatile("" : "+v"(t0), "+v"(t1), "+v"(t2));
        const unsigned t = i < K1 ? t0 : (i < 2 * K1 ? t1 : t2);
        lo = (blk0 * 8 + (t & ~7u) * gsz + (t & 7)) * TILEB;
        swz = ((lane & ~7u) | ((lane ^ (lane >> 3)) & 7)) * 16;
    }
    const unsigned cw = wave - 2, g = cw / 3, sl = cw - 3 * g;
    const unsigned coff = (lane & 7) * 16 + (lane & 8);
    const unsigned wbase = lbase + WOFF + sl * 24576;
    cv0 = (g * 64 + (lane >> 3)) * ROWB + sl * 128 + (lane & 7) * 16;
    cdv = (lane & 8) ? 7 * ROWB : 9 * ROWB;
    const unsigned list_l = full_addr + 256 + 4 * lane;
    if (wave == 0) {
        lds_st(full_addr + 4 * lane, lane >= 48 ? (unsigned)G : 0u);
        lds_st(list_l, lo + role * 4 * TILEB);
    }
    asm volatile("" : "+v"(lo), "+v"(swz), "+v"(cv0), "+v"(cdv));
    asm volatile("s_waitcnt lgkmcnt(0)\n\ts_barrier" :: "s"(x), "s"(w), "s"(bias), "s"(out), "s"(wsp) : "memory");

    u32x2 b2;
    if (wave >= 2) {
        asm volatile("global_load_dwordx2 %0, %1, %2" : "=v"(b2) : "v"(sl * 128 + coff), "s"(bias) : "memory");
        unsigned wl = wbase + g * 8192;
#pragma unroll 1
        for (int k = 0; k < 4; ++k) {
            dma_1k(w, cv0, wl); dma_1k(w, cv0 + cdv, wl + 1024);
            cv0 += 16 * ROWB; wl += 2048;
        }
    }
    GAS unsigned* mail = (GAS unsigned*)(wsp + pair * 256);
    unsigned m = 0;
    if (wave == 0) {
        __hip_atomic_store(mail + 32 * role, 0u, __ATOMIC_RELAXED, __HIP_MEMORY_SCOPE_AGENT);
        const unsigned long long pa = ((unsigned long long)(role ? x : out) + ((unsigned)__builtin_amdgcn_readlane(lo, K1 - 1) + role * 4 * TILEB)) | 8191ull;
        unsigned oa = (((unsigned)pa >> 2) ^ ((unsigned)pa >> 10)) & 2048u, ob = oa ^ 2048u;
        int score = 0;
#pragma unroll 1
        for (int r = 0; r < ROUNDS; ++r) {
            unsigned long long t0, t1; unsigned d0, d1;
            asm volatile("s_load_dword %2, %4, %5\n\ts_load_dword %3, %4, %6\n\t"
                         "s_waitcnt lgkmcnt(1)\n\ts_memtime %0\n\ts_waitcnt lgkmcnt(0)\n\ts_memtime %1\n\ts_waitcnt lgkmcnt(0)"
                         : "=&s"(t0), "=&s"(t1), "=&s"(d0), "=&s"(d1) : "s"(pa - 2047), "s"(oa), "s"(ob) : "memory");
            score = min((int)((unsigned)t1 - (unsigned)t0), 400) - score;
            const unsigned o = oa; oa = ob + 0x180; ob = o + 0x180;
        }
        m = (score < 0 ? 2u : 3u) | (abs(score) >= CONF ? 4u : 0u);
        __hip_atomic_store(mail + 32 - 32 * role, m, __ATOMIC_RELAXED, __HIP_MEMORY_SCOPE_AGENT);
    }
    int wave2 = wave;
    asm volatile("" : "+s"(wave2), "+s"(m));
    if (__builtin_expect(wave2 < 2, 1)) {
        if (__builtin_expect(wave2 == 1, 1)) {
            unsigned list = lo + role * 4 * TILEB;
            unsigned total = 64, known = K1, issued = 0, pub = 0, slot = lbase, tl = 0, te = 0, sa, sb, fly, vt, vd;
            asm volatile(
                "s_setprio 2\n"
                "1:\n\t"
                "s_cmp_lt_u32 %[issued], %[nfree] - 1\n\t"
                "s_cbranch_scc1 2f\n\t"
                "s_memrealtime s[60:61]\n"
                "2:\n\t"
                "ds_read_b32 %[vd], %[donel]\n\t"
                "s_getreg_b32 %[sa], hwreg(HW_REG_IB_STS)\n\t"
                "s_and_b32 %[sb], %[sa], 15\n\t"
                "s_lshr_b32 %[sa], %[sa], 18\n\t"
                "s_and_b32 %[sa], %[sa], 48\n\t"
                "s_or_b32 %[sa], %[sa], %[sb]\n\t"
                "s_mul_i32 %[sa], %[sa], 43\n\t"
                "s_addk_i32 %[sa], 215\n\t"
                "s_lshr_b32 %[fly], %[sa], 8\n\t"
                "s_sub_u32 %[sb], %[issued], %[fly]\n\t"
                "s_min_i32 %[sb], %[sb], %[total]\n\t"
                "s_cmp_gt_i32 %[sb], %[pub]\n\t"
                "s_cbranch_scc0 4f\n\t"
                "s_lshl3_add_u32 %[sa], %[fly], 12\n\t"
                "s_getpc_b64 s[62:63]\n\t"
                "s_add_u32 s62, s62, %[sa]\n\t"
                "s_addc_u32 s63, s63, 0\n\t"
                "s_setpc_b64 s[62:63]\n\t"
                "s_waitcnt vmcnt(0)\n\ts_branch 3f\n\ts_waitcnt vmcnt(6)\n\ts_branch 3f\n\ts_waitcnt vmcnt(12)\n\ts_branch 3f\n\t"
                "s_waitcnt vmcnt(18)\n\ts_branch 3f\n\ts_waitcnt vmcnt(24)\n\ts_branch 3f\n\ts_waitcnt vmcnt(30)\n\ts_branch 3f\n\t"
                "s_waitcnt vmcnt(36)\n"
                "3:\n\t"
                "s_mov_b32 %[pub], %[sb]\n\t"
                "s_cmp_eq_u32 %[pub], %[total]\n\t"
                "s_cselect_b32 %[sa], 0x80000000, 0\n\t"
                "v_mov_b32 %[vt], %[pub]\n\t"
                "v_or_b32 %[vt], %[sa], %[vt]\n\t"
                "ds_write_b32 %[fulla], %[vt]\n\t"
                "s_cbranch_scc0 4f\n\t"
                "s_endpgm\n"
                "4:\n\t"
                "s_waitcnt lgkmcnt(0)\n\t"
                "s_cmp_lt_u32 %[issued], %[known]\n\t"
                "s_cbranch_scc0 6f\n\t"
                "s_cmp_lt_u32 %[fly], %[depth]\n\t"
                "s_cbranch_scc0 5f\n\t"
                "s_cmp_lt_u32 %[issued], %[nfree]\n\t"
                "s_cselect_b32 %[tl], s60, %[tl]\n\t"
                "s_cselect_b32 %[te], s60, %[te]\n\t"
                "s_sub_u32 %[sb], s60, %[te]\n\t"
                "s_sub_u32 %[sa], s60, %[tl]\n\t"
                "s_or_b32 %[sb], %[sb], %[sa]\n\t"
                "s_cmp_ge_i32 %[sb], 0\n\t"
                "s_cbranch_scc0 5f\n\t"
                "s_sub_u32 %[sb], %[issued], %[lead]\n\t"
                "v_cmp_ge_i32 vcc, %[sb], %[vd]\n\t"
                "s_cbranch_vccnz 5f\n\t"
                "v_readlane_b32 %[sb], %[list], %[issued]\n\t"
                "s_sub_u32 %[sa], %[sa], %[allow]\n\t"
                "s_max_i32 %[sa], %[sa], 0\n\t"
                "s_add_u32 %[tl], %[tl], %[sa]\n\t"
                "s_add_u32 %[tl], %[tl], %[tau]\n\t"
                "s_add_u32 %[te], s60, %[tmin]\n\t"
                "s_add_u32 s62, %[xlo], %[sb]\n\t"
                "s_addc_u32 s63, %[xhi], 0\n\t"
                "s_mov_b32 m0, %[slot]\n\t"
                "s_add_u32 %[issued], %[issued], 1\n\t"
                "global_load_lds_dwordx4 %[swz], s[62:63] nt\n\t"
                "global_load_lds_dwordx4 %[swz], s[62:63] offset:1024 nt\n\t"
                "global_load_lds_dwordx4 %[swz], s[62:63] offset:2048 nt\n\t"
                "s_addk_i32 m0, 3072\n\t"
                "s_addk_i32 %[slot], %[tileb]\n\t"
                "global_load_lds_dwordx4 %[swz2], s[62:63] nt\n\t"
                "global_load_lds_dwordx4 %[swz2], s[62:63] offset:1024 nt\n\t"
                "global_load_lds_dwordx4 %[swz2], s[62:63] offset:2048 nt\n\t"
                "s_cmp_eq_u32 %[slot], %[rend]\n\t"
                "s_cselect_b32 %[slot], %[rbeg], %[slot]\n\t"
                "s_branch 1b\n"
                "6:\n\t"
                "s_cmp_lg_u32 %[known], %[k1]\n\t"
                "s_cbranch_scc1 5f\n\t"
                "ds_read_b32 %[vt], %[fulla] offset:4\n\t"
                "ds_read_b32 %[list], %[lista]\n\t"
                "s_waitcnt lgkmcnt(0)\n\t"
                "v_readfirstlane_b32 %[sa], %[vt]\n\t"
                "s_cmp_eq_u32 %[sa], 0\n\t"
                "s_cbranch_scc1 5f\n\t"
                "s_mov_b32 %[total], %[sa]\n\t"
                "s_add_u32 %[known], %[sa], %[extra]\n"
                "5:\n\t"
                "s_sleep 1\n\t"
                "s_branch 1b"
                : [total] "+&s"(total), [known] "+&s"(known), [issued] "+&s"(issued), [pub] "+&s"(pub), [slot] "+&s"(slot), [tl] "+&s"(tl), [te] "+&s"(te),
                  [list] "+&v"(list), [sa] "=&s"(sa), [sb] "=&s"(sb), [fly] "=&s"(fly), [vt] "=&v"(vt), [vd] "=&v"(vd)
                : [donel] "v"(full_addr + 64 + 4 * (lane < NCONS ? lane : 0)), [fulla] "v"(full_addr), [lista] "v"(list_l), [swz] "v"(swz), [swz2] "v"(swz + 3072),
                  [xlo] "s"((unsigned)(uintptr_t)x), [xhi] "s"((unsigned)((uintptr_t)x >> 32)), [rbeg] "s"(lbase), [rend] "s"(lbase + RING * TILEB),
                  [depth] "n"(DEPTH), [k1] "n"(K1), [extra] "n"(EXTRA), [lead] "n"(LEAD), [allow] "n"(ALLOW), [tau] "n"(TAU_LD), [tmin] "n"(TAU_MIN), [nfree] "n"(NFREE), [tileb] "n"(TILEB)
                : "memory", "scc", "vcc", "s60", "s61", "s62", "s63");
            return;
        }
        unsigned mo, cnt, sa, sb, va, vb, vc;
        asm volatile(
            "v_mov_b32 %[vc], %[m]\n\t"
            "s_mov_b32 %[cnt], 64\n\t"
            "s_waitcnt vmcnt(0)\n"
            "1:\n\t"
            "global_store_dword %[oth], %[vc], %[mail] sc1\n\t"
            "global_load_dword %[va], %[own], %[mail] sc1\n\t"
            "s_sub_u32 %[cnt], %[cnt], 1\n\t"
            "s_waitcnt vmcnt(0)\n\t"
            "v_readfirstlane_b32 %[mo], %[va]\n\t"
            "s_cbranch_scc1 2f\n\t"
            "s_cmp_lt_u32 %[mo], 2\n\t"
            "s_cbranch_scc1 1b\n"
            "2:\n\t"
            "s_mov_b32 vcc_lo, %[bylo]\n\t"
            "s_mov_b32 vcc_hi, -1\n\t"
            "s_sub_u32 %[sa], %[role], 1\n\t"
            "s_xor_b32 vcc_lo, vcc_lo, %[sa]\n\t"
            "s_xor_b32 vcc_hi, vcc_hi, %[sa]\n\t"
            "s_bfe_i32 %[sa], %[m], 0x10002\n\t"
            "s_xor_b32 %[sb], %[m], %[mo]\n\t"
            "s_bitcmp1_b32 %[sb], 2\n\t"
            "s_cselect_b32 vcc_lo, %[sa], vcc_lo\n\t"
            "s_cselect_b32 vcc_hi, %[sa], vcc_hi\n\t"
            "s_not_b32 %[sb], %[mo]\n\t"
            "v_mov_b32 %[va], %[pother]\n\t"
            "v_mov_b32 %[vb], %[pown]\n\t"
            "v_cndmask_b32 %[va], %[va], %[vb], vcc\n\t"
            "v_add_u32 %[va], %[va], %[lo]\n\t"
            "v_lshrrev_b32 %[vb], 8, %[va]\n\t"
            "v_xor_b32 %[va], %[va], %[vb]\n\t"
            "v_lshrrev_b32 %[va], 13, %[va]\n\t"
            "v_mov_b32 %[vb], %[sb]\n\t"
            "v_cndmask_b32 %[vb], %[vb], %[vc], vcc\n\t"
            "v_xor_b32 %[va], %[va], %[vb]\n\t"
            "v_and_b32 %[va], 1, %[va]\n\t"
            "v_mul_u32_u24 %[va], %[t4], %[va]\n\t"
            "v_add_u32 %[vb], %[lo], %[va]\n\t"
            "s_mov_b32 exec_lo, %[k1m]\n\t"
            "ds_write_b32 %[listl], %[vb]\n\t"
            "v_xor_b32 %[va], %[t4], %[va]\n\t"
            "v_add_u32 %[vb], %[lo], %[va]\n\t"
            "s_cmp_lt_u32 %[mo], 2\n\t"
            "s_cselect_b32 %[sa], %[nboth], %[nt]\n\t"
            "s_cbranch_scc0 3f\n\t"
            "s_mov_b32 exec_hi, %[ntm]\n\t"
            "ds_write_b32 %[listl], %[vb] offset:%[o2]\n"
            "3:\n\t"
            "v_mov_b32 %[va], %[sa]\n\t"
            "ds_write_b32 %[fulla], %[va] offset:4\n"
            "4:\n\t"
            "s_sleep 12\n\t"
            "global_store_dword %[oth], %[vc], %[mail] sc1\n\t"
            "ds_read_b32 %[va], %[fulla]\n\t"
            "s_waitcnt lgkmcnt(0)\n\t"
            "v_cmp_gt_u32 vcc, %[nt] - 6, %[va]\n\t"
            "s_cbranch_vccnz 4b\n\t"
            "s_endpgm"
            : [mo] "=&s"(mo), [cnt] "=&s"(cnt), [sa] "=&s"(sa), [sb] "=&s"(sb), [va] "=&v"(va), [vb] "=&v"(vb), [vc] "=&v"(vc)
            : [m] "s"(m), [role] "s"(role), [mail] "s"(mail), [own] "v"(role * 128), [oth] "v"(128 - role * 128), [lo] "v"(lo), [listl] "v"(list_l), [fulla] "v"(full_addr),
              [pown] "s"((unsigned)(uintptr_t)(role ? x : out) + TILEB / 2), [pother] "s"((unsigned)(uintptr_t)(role ? out : x) + TILEB / 2),
              [bylo] "n"((int)(0xaaaaaaaau | (~0u << K3))), [k1m] "n"((int)(~0u << K1)), [ntm] "n"((1 << (NT - 32)) - 1), [t4] "n"(4 * TILEB),
              [nboth] "n"(2 * NT - K1), [nt] "n"(NT), [o2] "n"(4 * (NT - K1))
            : "memory", "scc", "vcc");
        return;
    }

    const unsigned c = lane & 15, q = lane >> 4;
    unsigned aoff[6];
#pragma unroll
    for (int s = 0; s < 6; s += 2) {
        const unsigned line = 3 * c + (s >> 1);
        aoff[s] = lbase + line * 128 + ((q ^ line) & 7) * 16;
        aoff[s + 1] = aoff[s] ^ 64;
    }
    unsigned o4 = (4 * q + (c >> 3)) * ROWB + sl * 128 + 16 * (c & 7);
    unsigned wr_l = full_addr + 128 + 4 * (sl + 3 * (lane < G ? lane : 0));
    unsigned we = wbase + (8 * q + (q & 1)) * 128 + coff, wo = wbase + (8 * q - (q & 1)) * 128 + coff;
    unsigned need = lane < g ? 2u : 1u;
    unsigned done_addr = full_addr + 64 + 4 * cw, next_addr = full_addr + 192 + 4 * sl;
    asm volatile("" : "+v"(aoff[0]), "+v"(aoff[1]), "+v"(aoff[2]), "+v"(aoff[3]), "+v"(aoff[4]), "+v"(aoff[5]), "+v"(o4), "+v"(wr_l), "+v"(we), "+v"(wo),
                      "+v"(need), "+v"(done_addr), "+v"(next_addr));
    asm volatile("s_waitcnt vmcnt(0)\n\tds_write_b32 %1, %2 offset:64" : "+v"(b2) : "v"(done_addr), "v"(1u) : "memory");
    while (__builtin_amdgcn_ballot_w64(lds_ld(wr_l) < need) != 0) {}
    u32x4 wf[6][4];
    {
        u32x2 r2[6][8];
#define WRD(S) lds_ld64<(32 * S + 0) * 128>(r2[S][0], we); lds_ld64<(32 * S + 1) * 128>(r2[S][1], wo); lds_ld64<(32 * S + 2) * 128>(r2[S][2], we); lds_ld64<(32 * S + 3) * 128>(r2[S][3], wo); \
               lds_ld64<(32 * S + 4) * 128>(r2[S][4], we); lds_ld64<(32 * S + 5) * 128>(r2[S][5], wo); lds_ld64<(32 * S + 6) * 128>(r2[S][6], we); lds_ld64<(32 * S + 7) * 128>(r2[S][7], wo);
#define WWAIT(S, N) asm volatile("s_waitcnt lgkmcnt(" #N ")" : "+v"(r2[S][0]), "+v"(r2[S][1]), "+v"(r2[S][2]), "+v"(r2[S][3]), "+v"(r2[S][4]), "+v"(r2[S][5]), "+v"(r2[S][6]), "+v"(r2[S][7]) :: "memory");
#define WPERM(S) _Pragma("unroll") for (int t = 0; t < 4; ++t) _Pragma("unroll") for (int jj = 0; jj < 4; ++jj) \
                     wf[S][t][jj] = __builtin_amdgcn_perm(r2[S][2 * jj + 1][t >> 1], r2[S][2 * jj][t >> 1], (t & 1) ? 0x07060302u : 0x05040100u); \
                 asm volatile("" : "+v"(wf[S][0]), "+v"(wf[S][1]), "+v"(wf[S][2]), "+v"(wf[S][3]));
        WRD(0) WRD(1)
        WWAIT(0, 8) WRD(2) WPERM(0)
        WWAIT(1, 8) WRD(3) WPERM(1)
        WWAIT(2, 8) WRD(4) WPERM(2)
        WWAIT(3, 8) WRD(5)
        asm volatile("ds_write_b32 %0, %1 offset:64" :: "v"(done_addr), "v"(2u) : "memory");
        WPERM(3)
        WWAIT(4, 9) WPERM(4)
        WWAIT(5, 0) WPERM(5)
#undef WRD
#undef WWAIT
#undef WPERM
    }
    f32x4 bv[4];
#pragma unroll
    for (int t = 0; t < 4; ++t) {
        const float f = __builtin_bit_cast(float, (t & 1) ? (b2[t >> 1] & 0xffff0000u) : (b2[t >> 1] << 16));
        bv[t] = f32x4{f, f, f, f};
    }
    const __amdgpu_buffer_rsrc_t orsrc = __builtin_amdgcn_make_buffer_rsrc(out, 0, M_FAST * ROWB, 0x00020000);
    unsigned full = 0, ts = 0, tc = 0;
#pragma unroll 1
    for (unsigned i = g;;) {
        {
            unsigned vt, sa;
            asm volatile("s_cmp_gt_u32 %[full], %[i]\n\t"
                         "s_cbranch_scc1 2f\n"
                         "1:\n\t"
                         "ds_read_b32 %[vt], %[fa]\n\t"
                         "s_waitcnt lgkmcnt(0)\n\t"
                         "v_readfirstlane_b32 %[sa], %[vt]\n\t"
                         "s_and_b32 %[full], %[sa], 0xffff\n\t"
                         "s_cmp_gt_u32 %[full], %[i]\n\t"
                         "s_cbranch_scc1 2f\n\t"
                         "s_sleep 1\n\t"
                         "s_cmp_ge_i32 %[sa], 0\n\t"
                         "s_cbranch_scc1 1b\n\t"
                         "s_endpgm\n"
                         "3:\n\t"
                         "s_sleep 1\n"
                         "2:\n\t"
                         "s_cmp_ge_u32 %[i], %[npace]\n\t"
                         "s_cbranch_scc1 4f\n\t"
                         "s_memrealtime s[60:61]\n\t"
                         "s_cmp_lt_u32 %[i], %[g]\n\t"
                         "s_waitcnt lgkmcnt(0)\n\t"
                         "s_cselect_b32 %[ts], s60, %[ts]\n\t"
                         "s_cselect_b32 %[tc], s60, %[tc]\n\t"
                         "s_sub_u32 s61, s60, %[tc]\n\t"
                         "s_sub_u32 %[sa], s60, %[ts]\n\t"
                         "s_or_b32 s61, s61, %[sa]\n\t"
                         "s_cmp_lt_i32 s61, 0\n\t"
                         "s_cbranch_scc1 3b\n\t"
                         "s_sub_u32 %[sa], %[sa], %[allow]\n\t"
                         "s_max_i32 %[sa], %[sa], 0\n\t"
                         "s_add_u32 %[ts], %[ts], %[sa]\n\t"
                         "s_cmp_lt_u32 %[i], %[g]\n\t"
                         "s_cselect_b32 %[sa], %[tau1], %[tau]\n\t"
                         "s_add_u32 %[ts], %[ts], %[sa]\n\t"
                         "s_add_u32 %[tc], s60, %[tmin]\n"
                         "4:"
                         : [full] "+&s"(full), [ts] "+&s"(ts), [tc] "+&s"(tc), [vt] "=&v"(vt), [sa] "=&s"(sa)
                         : [i] "s"(i), [fa] "v"(full_addr), [npace] "n"(NPACED), [g] "n"(G), [allow] "n"(ALLOW), [tau] "s"(G * TAU_C), [tau1] "n"(G * TAU_C + SLOW), [tmin] "n"(G * TAU_MIN)
                         : "memory", "scc", "s60", "s61");
        }
        const unsigned sb = (i % RING) * TILEB;
        u32x4 a[6];
#pragma unroll
        for (int s = 0; s < 6; ++s) a[s] = *(LAS u32x4*)(uintptr_t)(aoff[s] + sb);
        const unsigned toff = *(LAS unsigned*)(uintptr_t)(full_addr + 256 + 4 * i);
        unsigned nxt_v;
        asm volatile("s_mov_b64 exec, 1\n\tds_add_rtn_u32 %0, %1, %2\n\ts_mov_b64 exec, -1" : "=v"(nxt_v) : "v"(next_addr), "v"(1u) : "memory");
        __builtin_amdgcn_sched_barrier(0);
        f32x4 acc[4];
#pragma unroll
        for (int t = 0; t < 4; ++t) acc[t] = __builtin_amdgcn_mfma_f32_16x16x32_bf16(__builtin_bit_cast(bf16x8, a[0]), __builtin_bit_cast(bf16x8, wf[0][t]), bv[t], 0, 0, 0);
#pragma unroll
        for (int s = 1; s < 6; ++s)
#pragma unroll
            for (int t = 0; t < 4; ++t) acc[t] = __builtin_amdgcn_mfma_f32_16x16x32_bf16(__builtin_bit_cast(bf16x8, a[s]), __builtin_bit_cast(bf16x8, wf[s][t]), acc[t], 0, 0, 0);
        __builtin_amdgcn_sched_barrier(0);
        asm volatile("s_waitcnt lgkmcnt(0)" : "+v"(nxt_v) :: "memory");
        i = __builtin_amdgcn_readfirstlane(nxt_v);
        lds_st(done_addr, i);
        const unsigned ov = o4 + toff;
#pragma unroll
        for (int r = 0; r < 4; r += 2) {
            const unsigned e0 = pk_bf16(acc[0][r], acc[1][r]), e1 = pk_bf16(acc[2][r], acc[3][r]);
            const unsigned f0 = pk_bf16(acc[0][r + 1], acc[1][r + 1]), f1 = pk_bf16(acc[2][r + 1], acc[3][r + 1]);
            u32x4 v4;
            v4[0] = __builtin_amdgcn_update_dpp(e0, f0, 0x128, 0xf, 0xc, false);
            v4[1] = __builtin_amdgcn_update_dpp(e1, f1, 0x128, 0xf, 0xc, false);
            v4[2] = __builtin_amdgcn_update_dpp(f0, e0, 0x128, 0xf, 0x3, false);
            v4[3] = __builtin_amdgcn_update_dpp(f1, e1, 0x128, 0xf, 0x3, false);
            __builtin_amdgcn_raw_buffer_store_b128(v4, orsrc, ov + r * ROWB, 0, 16);
        }
    }
}

__global__ void conv1x1_generic(const __hip_bfloat16* __restrict__ x, const __hip_bfloat16* __restrict__ w,
                                const __hip_bfloat16* __restrict__ b, __hip_bfloat16* __restrict__ out, long total, int cin, int cout) {
    for (long idx = (long)blockIdx.x * 256 + threadIdx.x; idx < total; idx += 2048L * 256) {
        const long m = idx / cout; const int n = (int)(idx - m * cout);
        float acc = 0.f;
        for (int k = 0; k < cin; ++k) acc += __bfloat162float(x[m * cin + k]) * __bfloat162float(w[(long)k * cout + n]);
        out[idx] = __float2bfloat16(acc + __bfloat162float(b[n]));
    }
}

extern "C" void kernel_launch(void* const* d_in, const int* in_sizes, int n_in,
                              void* d_out, int out_size, void* d_ws, size_t ws_size,
                              hipStream_t stream) {
    const int cout = in_sizes[2];
    const bool aligned = (((uintptr_t)d_in[0] | (uintptr_t)d_in[1] | (uintptr_t)d_in[2] | (uintptr_t)d_out | (uintptr_t)d_ws) & 15) == 0;
    if (cout == C && in_sizes[0] == M_FAST * C && in_sizes[1] == C * C && out_size == M_FAST * C && ws_size >= 128 * 256 && aligned) {
        conv1x1_fast<<<NWG, NTHREADS, 0, stream>>>((const char*)d_in[0], (const char*)d_in[1], (const char*)d_in[2], (char*)d_out, (char*)d_ws);
    } else {
        conv1x1_generic<<<2048, 256, 0, stream>>>((const __hip_bfloat16*)d_in[0], (const __hip_bfloat16*)d_in[1], (const __hip_bfloat16*)d_in[2],
                                                   (__hip_bfloat16*)d_out, (long)out_size, in_sizes[1] / cout, cout);
    }
}
```
